```python
import torch

def _softmax(x, axis=-1):
    return torch.softmax(x, dim=axis)
def _gelu(x):
    return torch.nn.functional.gelu(x, approximate="tanh")
def _silu(x):
    return torch.nn.functional.silu(x)
def _sigmoid(x):
    return torch.sigmoid(x)
def _layernorm(x, axis=-1, eps=1e-5):
    m = x.mean(dim=axis, keepdim=True)
    v = x.var(dim=axis, keepdim=True, unbiased=False)
    return (x - m) / torch.sqrt(v + eps)
def _scatter_add(tgt, idx, src, axis=0):
    return tgt.clone().index_add_(axis, idx, src)
def _segment_sum(src, seg, n):
    out = torch.zeros((n,) + src.shape[1:], dtype=src.dtype)
    return out.index_add_(0, seg, src)
def _pad(x, pw):
    flat = tuple(v for pair in reversed(pw) for v in pair)
    return torch.nn.functional.pad(x, flat)

kernel_name = 'gated_mix__linear_kernel_hedgehog__mixer_s_mlp__prompt'

def setup_inputs():

    _w = (lambda a: (lambda t: t.to(torch.bfloat16) if t.is_floating_point() else t)(torch.as_tensor(a)))
    def _A_setup():
        B, S, D, H = 1, 2048, 1024, 32
        Dh = D // H
        return {
            "x": _w((lambda *s: torch.randn(*s, dtype=torch.bfloat16) * 0.05)(B, S, D)),
            "wqkv": _w((lambda *s: torch.randn(*s, dtype=torch.bfloat16) * 1.6)(D, 3 * D)),
            "w_phi": _w((lambda *s: torch.randn(*s, dtype=torch.bfloat16) * 1.6)(H, Dh, Dh) * (Dh ** -0.5)),
            "wo": _w((lambda *s: torch.randn(*s, dtype=torch.bfloat16) * 0.8)(D, D)),
        }
    _A_in = _A_setup()

    def _B_setup():
        B, S, D, F = 1, 2048, 1024, 3584
        return {
            "x":  _w((lambda *s: torch.randn(*s, dtype=torch.bfloat16) * 0.05)(B, S, D)),
            "w1": _w((lambda *s: torch.randn(*s, dtype=torch.bfloat16) * 3.2)(D, D) * (D ** -0.5)),
            "w2": _w((lambda *s: torch.randn(*s, dtype=torch.bfloat16) * 3.2)(D, D) * (D ** -0.5)),
            "w3": _w((lambda *s: torch.randn(*s, dtype=torch.bfloat16) * 3.2)(D, F) * (D ** -0.5)),
            "w4": _w((lambda *s: torch.randn(*s, dtype=torch.bfloat16) * 1.6)(F, D) * (F ** -0.5)),
        }
    _B_in = _B_setup()

    return {
        "A_x": _A_in["x"],
        "A_wqkv": _A_in["wqkv"],
        "A_w_phi": _A_in["w_phi"],
        "A_wo": _A_in["wo"],
        "B_x": _B_in["x"],
        "B_w1": _B_in["w1"],
        "B_w2": _B_in["w2"],
        "B_w3": _B_in["w3"],
        "B_w4": _B_in["w4"],
        "gate_w": _w((lambda *s: torch.randn(*s, dtype=torch.bfloat16) * 0.05)(1024) * 16.0),
    }

def reference(A_x, A_wqkv, A_w_phi, A_wo, B_x, B_w1, B_w2, B_w3, B_w4, gate_w):
    def _A_ref(x, wqkv, w_phi, wo):
        B, S, D = x.shape
        H = 32; Dh = D // H
        qkv = (lambda x, *d: x.permute(*d))((x @ wqkv).reshape(B, S, 3, H, Dh), 2, 0, 3, 1, 4)
        q, k, v = qkv[0], qkv[1], qkv[2]
        pq = torch.einsum("bhsd,hdf->bhsf", q, w_phi)
        pk = torch.einsum("bhsd,hdf->bhsf", k, w_phi)
        phi_q = torch.concatenate([_softmax(pq, axis=-1), _softmax(-pq, axis=-1)], axis=-1)
        phi_k = torch.concatenate([_softmax(pk, axis=-1), _softmax(-pk, axis=-1)], axis=-1)
        kv = torch.einsum("bhsf,bhsd->bhfd", phi_k, v)
        z  = torch.einsum("bhsf->bhf", phi_k)
        num = torch.einsum("bhqf,bhfd->bhqd", phi_q, kv)
        den = torch.einsum("bhqf,bhf->bhq", phi_q, z)[..., None] + 1e-6
        o = num / den
        return (lambda x, *d: x.permute(*d))(o, 0, 2, 1, 3).reshape(B, S, D) @ wo

    def _B_ref(x, w1, w2, w3, w4):
        B, S, D = x.shape
        h = _gelu(_layernorm(x, axis=-1) @ w1)
        c = D // 4
        g0 = _pad(h[:, :, 0*c:1*c], ((0, 0), (1, 0), (0, 0)))[:, :S, :]
        g1 = _pad(h[:, :, 1*c:2*c], ((0, 0), (0, 1), (0, 0)))[:, 1:, :]
        g2 = _pad(h[:, :, 2*c:3*c], ((0, 0), (2, 0), (0, 0)))[:, :S, :]
        g3 = _pad(h[:, :, 3*c:   ], ((0, 0), (0, 2), (0, 0)))[:, 2:, :]
        hs = torch.concatenate([g0, g1, g2, g3], axis=-1) @ w2
        x1 = x + hs
        h2 = _gelu(_layernorm(x1, axis=-1) @ w3) @ w4
        return x1 + h2

    x = A_x
    g = _sigmoid((x * gate_w).sum(-1, keepdims=True))
    a = _A_ref(x, A_wqkv, A_w_phi, A_wo)
    b = _B_ref(x, B_w1, B_w2, B_w3, B_w4)
    return g * a + (1 - g) * b
```

```cpp
#include <hip/hip_runtime.h>
#include <hip/hip_bf16.h>
#include <stdint.h>

typedef __attribute__((ext_vector_type(8))) short bf16x8;
typedef __attribute__((ext_vector_type(4))) short bf16x4;
typedef __attribute__((ext_vector_type(4))) float f32x4;
typedef unsigned short u16;
#define LDS_AS __attribute__((address_space(3)))

constexpr int S_ = 2048, D_ = 1024, F_ = 3584;

__device__ __forceinline__ float bf2f(u16 u) { return __uint_as_float(((unsigned)u) << 16); }
__device__ __forceinline__ u16 f2bf(float f) {
  __hip_bfloat16 b = __float2bfloat16(f);
  return *reinterpret_cast<u16*>(&b);
}
__device__ __forceinline__ float rbf(float f) { return bf2f(f2bf(f)); }
__device__ __forceinline__ float gelu_t(float x) {
  float u = 0.7978845608028654f * (x + 0.044715f * x * x * x);
  float e = __expf(2.f * u);
  float t = 1.f - 2.f / (1.f + e);
  return 0.5f * x * (1.f + t);
}

constexpr size_t OFF_PHIQ = 0;
constexpr size_t OFF_KVS = OFF_PHIQ + (size_t)2048 * 2048 * 2;
constexpr size_t OFF_ZS = OFF_KVS + (size_t)32 * 32 * 2048 * 4;
constexpr size_t OFF_H = OFF_ZS + (size_t)32 * 32 * 64 * 4;
constexpr size_t OFF_X1 = OFF_H + (size_t)2052 * 1024 * 2;
constexpr size_t OFF_ST2 = OFF_X1 + (size_t)2048 * 1024 * 2;
constexpr size_t OFF_O = OFF_ST2 + (size_t)2048 * 64 * 4;
constexpr size_t OFF_H3 = OFF_O + (size_t)2048 * 1024 * 2;
constexpr size_t OFF_GATE = OFF_H3 + (size_t)2048 * 3584 * 2;
constexpr size_t OFF_STX = OFF_GATE + 2048 * 4;
constexpr size_t OFF_CS1 = OFF_STX + 2048 * 2 * 4;
constexpr size_t OFF_CS3 = OFF_CS1 + 1024 * 4;

template <int BN>
__device__ __forceinline__ int bswz(int krow) {
  if constexpr (BN == 128) return ((krow & 3) | (((krow >> 3) & 1) << 2)) << 1;
  else return (((krow >> 1) & 1) | (((krow >> 3) & 1) << 1)) << 1;
}

template <int BM, int BN, int WM, int WN, bool SHIFT>
__device__ __forceinline__ void mainloop(f32x4 (&acc)[BM / WM / 16][BN / WN / 16],
                                         const u16* const (&asrc)[BM / 8 / (WM * WN)], int lda,
                                         const u16* const (&bsrc)[BN / 8 / (WM * WN)], int ldb, int K,
                                         char* smem, int wid, int lane) {
  constexpr int NW = WM * WN, TM = BM / WM / 16, TN = BN / WN / 16;
  constexpr int NAI = BM / 8 / NW, NBI = BN / 8 / NW;
  constexpr int ABYTES = BM * 128, BBYTES = 64 * BN * 2, STAGE = ABYTES + BBYTES;
  constexpr int BROW = BN * 2;
  const int wr = wid / WN, wc = wid % WN, fr = lane & 15, fq = lane >> 4;
  const int nt = K / 64;
  auto stage = [&](int t, int buf) {
    char* la = smem + buf * STAGE;
    char* lb = la + ABYTES;
    long aoff = (long)t * 64;
    if constexpr (SHIFT) {
      int g = t >> 2;
      int dl = (g == 0) ? -1 : (g == 1) ? 1 : (g == 2) ? -2 : 2;
      aoff += (long)dl * lda;
    }
#pragma unroll
    for (int i = 0; i < NAI; ++i)
      __builtin_amdgcn_global_load_lds((const unsigned*)(asrc[i] + aoff), (unsigned*)(la + (wid * NAI + i) * 1024), 16, 0, 0);
    long boff = (long)t * 64 * ldb;
#pragma unroll
    for (int i = 0; i < NBI; ++i)
      __builtin_amdgcn_global_load_lds((const unsigned*)(bsrc[i] + boff), (unsigned*)(lb + (wid * NBI + i) * 1024), 16, 0, 0);
  };
  stage(0, 0);
  __syncthreads();
  for (int t = 0; t < nt; ++t) {
    int buf = t & 1;
    if (t + 1 < nt) stage(t + 1, buf ^ 1);
    const char* la = smem + buf * STAGE;
    const char* lb = la + ABYTES;
#pragma unroll
    for (int ks = 0; ks < 2; ++ks) {
      bf16x8 af[TM];
      bf16x8 bfr[TN];
#pragma unroll
      for (int m = 0; m < TM; ++m) {
        int row = wr * (TM * 16) + m * 16 + fr;
        af[m] = *reinterpret_cast<const bf16x8*>(la + row * 128 + (((ks * 4 + fq) ^ (row & 7)) << 4));
      }
#pragma unroll
      for (int n = 0; n < TN; ++n) {
        int col = wc * (TN * 16) + n * 16 + (fr & 3) * 4;
        bf16x4 lo, hi;
        {
          int krow = ks * 32 + fq * 8 + (fr >> 2);
          const char* p = lb + krow * BROW + ((((col >> 3)) ^ bswz<BN>(krow)) << 4) + ((col & 4) << 1);
          lo = __builtin_amdgcn_ds_read_tr16_b64_v4i16((LDS_AS bf16x4*)p);
        }
        {
          int krow = ks * 32 + fq * 8 + 4 + (fr >> 2);
          const char* p = lb + krow * BROW + ((((col >> 3)) ^ bswz<BN>(krow)) << 4) + ((col & 4) << 1);
          hi = __builtin_amdgcn_ds_read_tr16_b64_v4i16((LDS_AS bf16x4*)p);
        }
        bfr[n] = __builtin_shufflevector(lo, hi, 0, 1, 2, 3, 4, 5, 6, 7);
      }
#pragma unroll
      for (int m = 0; m < TM; ++m)
#pragma unroll
        for (int n = 0; n < TN; ++n)
          acc[m][n] = __builtin_amdgcn_mfma_f32_16x16x32_bf16(af[m], bfr[n], acc[m][n], 0, 0, 0);
    }
    __syncthreads();
  }
}

template <int BM, int NW>
__device__ __forceinline__ void make_asrc(const u16* (&asrc)[BM / 8 / NW], const u16* Ablk, int lda, int wid, int lane) {
#pragma unroll
  for (int i = 0; i < BM / 8 / NW; ++i) {
    int row = 8 * (wid * (BM / 8 / NW) + i) + (lane >> 3);
    int ch = (lane & 7) ^ (row & 7);
    asrc[i] = Ablk + (long)row * lda + ch * 8;
  }
}
template <int BN, int NW>
__device__ __forceinline__ void make_bsrc(const u16* (&bsrc)[BN / 8 / NW], const u16* W, int ldb, int colbase, int wid, int lane) {
  constexpr int LPR = BN / 8;
#pragma unroll
  for (int i = 0; i < BN / 8 / NW; ++i) {
    int krow = (64 / LPR) * (wid * (BN / 8 / NW) + i) + lane / LPR;
    int ch = (lane % LPR) ^ bswz<BN>(krow);
    bsrc[i] = W + (long)krow * ldb + colbase + ch * 8;
  }
}

__global__ __launch_bounds__(256) void k0_stats(const u16* __restrict__ x, const u16* __restrict__ gw,
                                                const u16* __restrict__ w1, const u16* __restrict__ w3,
                                                float* __restrict__ stx, float* __restrict__ gate,
                                                float* __restrict__ cs1, float* __restrict__ cs3, u16* __restrict__ hbuf) {
  __shared__ float red[16][128];
  int b = blockIdx.x, tid = threadIdx.x, lane = tid & 63, wid = tid >> 6;
  if (b < 512) {
    int row = b * 4 + wid;
    const u16* xr = x + (long)row * D_;
    float s = 0.f, ss = 0.f, gs = 0.f;
#pragma unroll
    for (int i = 0; i < 2; ++i) {
      bf16x8 v = *reinterpret_cast<const bf16x8*>(xr + i * 512 + lane * 8);
      bf16x8 g = *reinterpret_cast<const bf16x8*>(gw + i * 512 + lane * 8);
#pragma unroll
      for (int j = 0; j < 8; ++j) {
        float f = bf2f((u16)v[j]);
        s += f;
        ss += f * f;
        gs += rbf(f * bf2f((u16)g[j]));
      }
    }
#pragma unroll
    for (int o = 1; o < 64; o <<= 1) {
      s += __shfl_xor(s, o);
      ss += __shfl_xor(ss, o);
      gs += __shfl_xor(gs, o);
    }
    if (lane == 0) {
      float m = s * (1.f / D_);
      float var = fmaxf(ss * (1.f / D_) - m * m, 0.f);
      stx[row * 2] = m;
      stx[row * 2 + 1] = rsqrtf(var + 1e-5f);
      float gg = rbf(gs);
      gate[row] = rbf(1.f / (1.f + __expf(-gg)));
    }
  } else if (b < 512 + 36) {
    int cb = b - 512;
    const u16* W;
    float* cs;
    int ldw, c0;
    if (cb < 8) { W = w1; cs = cs1; ldw = D_; c0 = cb * 128; }
    else { W = w3; cs = cs3; ldw = F_; c0 = (cb - 8) * 128; }
    int ksl = tid >> 4, ch = tid & 15;
    float a[8] = {0, 0, 0, 0, 0, 0, 0, 0};
    for (int r = 0; r < 64; ++r) {
      bf16x8 v = *reinterpret_cast<const bf16x8*>(W + (long)(ksl * 64 + r) * ldw + c0 + ch * 8);
#pragma unroll
      for (int j = 0; j < 8; ++j) a[j] += bf2f((u16)v[j]);
    }
#pragma unroll
    for (int j = 0; j < 8; ++j) red[ksl][ch * 8 + j] = a[j];
    __syncthreads();
    if (tid < 128) {
      float t = 0.f;
#pragma unroll
      for (int i = 0; i < 16; ++i) t += red[i][tid];
      cs[c0 + tid] = t;
    }
  } else {
    for (int i = tid; i < 4 * D_ / 8; i += 256) {
      int r = i / (D_ / 8), c = i % (D_ / 8);
      int rr = (r < 2) ? r : 2048 + r;
      bf16x8 z = {0, 0, 0, 0, 0, 0, 0, 0};
      *reinterpret_cast<bf16x8*>(hbuf + (long)rr * D_ + c * 8) = z;
    }
  }
}

__global__ __launch_bounds__(256) void k1_gemm(const u16* __restrict__ x, const u16* __restrict__ wqkv,
                                               const u16* __restrict__ wphi, const u16* __restrict__ w1,
                                               const float* __restrict__ stx, const float* __restrict__ cs1,
                                               u16* __restrict__ phiq, float* __restrict__ kvs, float* __restrict__ zs,
                                               u16* __restrict__ hbuf) {
  extern __shared__ __attribute__((aligned(16))) char smem[];
  constexpr int BM = 128, BN = 128, WM = 2, WN = 2, NW = 4;
  int b = blockIdx.x, tid = threadIdx.x, lane = tid & 63;
  int wid = __builtin_amdgcn_readfirstlane(tid >> 6);
  int ct = (b & 7) * 4 + ((b >> 3) & 3), rb = b >> 5;
  const int wr = wid >> 1, wc = wid & 1, fr = lane & 15, fq = lane >> 4;
  const u16* asrc[BM / 8 / NW];
  const u16* bsrc[BN / 8 / NW];
  make_asrc<BM, NW>(asrc, x + (long)rb * BM * D_, D_, wid, lane);
  int ldb;
  if (ct < 8) {
    ldb = 3 * D_;
    make_bsrc<BN, NW>(bsrc, wqkv, ldb, ct * 128, wid, lane);
  } else if (ct < 24) {
    ldb = 3 * D_;
    int hp = ct - 8;
#pragma unroll
    for (int i = 0; i < BN / 8 / NW; ++i) {
      int krow = 4 * (wid * (BN / 8 / NW) + i) + (lane >> 4);
      int ch = (lane & 15) ^ bswz<BN>(krow);
      int col = ch * 8;
      int gcol = (((col >> 5) & 1) ? 2048 : 1024) + (2 * hp + (col >> 6)) * 32 + (col & 31);
      bsrc[i] = wqkv + (long)krow * ldb + gcol;
    }
  } else {
    ldb = D_;
    make_bsrc<BN, NW>(bsrc, w1, ldb, (ct - 24) * 128, wid, lane);
  }
  f32x4 acc[4][4];
#pragma unroll
  for (int m = 0; m < 4; ++m)
#pragma unroll
    for (int n = 0; n < 4; ++n) acc[m][n] = f32x4{0.f, 0.f, 0.f, 0.f};
  mainloop<BM, BN, WM, WN, false>(acc, asrc, D_, bsrc, ldb, D_, smem, wid, lane);

  char* wl = smem + wid * 9216;
  const int grow0 = rb * BM + wr * 64;
  if (ct < 8) {
#pragma unroll
    for (int m = 0; m < 4; ++m)
#pragma unroll
      for (int n = 0; n < 4; ++n)
#pragma unroll
        for (int r = 0; r < 4; ++r)
          *reinterpret_cast<u16*>(wl + (m * 16 + fq * 4 + r) * 144 + (n * 16 + fr) * 2) = f2bf(acc[m][n][r]);
    bf16x8 af[2][4];
#pragma unroll
    for (int hh = 0; hh < 2; ++hh)
#pragma unroll
      for (int m = 0; m < 4; ++m)
        af[hh][m] = *reinterpret_cast<const bf16x8*>(wl + (m * 16 + fr) * 144 + (hh * 32 + fq * 8) * 2);
#pragma unroll
    for (int hh = 0; hh < 2; ++hh) {
      int h = ct * 4 + wc * 2 + hh;
      bf16x8 wb[2];
#pragma unroll
      for (int ft = 0; ft < 2; ++ft)
#pragma unroll
        for (int j = 0; j < 8; ++j) wb[ft][j] = (short)wphi[h * 1024 + (fq * 8 + j) * 32 + ft * 16 + fr];
#pragma unroll
      for (int m = 0; m < 4; ++m) {
        f32x4 p0 = {0.f, 0.f, 0.f, 0.f}, p1 = {0.f, 0.f, 0.f, 0.f};
        p0 = __builtin_amdgcn_mfma_f32_16x16x32_bf16(af[hh][m], wb[0], p0, 0, 0, 0);
        p1 = __builtin_amdgcn_mfma_f32_16x16x32_bf16(af[hh][m], wb[1], p1, 0, 0, 0);
#pragma unroll
        for (int r = 0; r < 4; ++r) {
          float v0 = rbf(p0[r]), v1 = rbf(p1[r]);
          float mx = fmaxf(v0, v1), mn = fminf(v0, v1);
#pragma unroll
          for (int o = 1; o < 16; o <<= 1) {
            mx = fmaxf(mx, __shfl_xor(mx, o));
            mn = fminf(mn, __shfl_xor(mn, o));
          }
          float e0 = __expf(v0 - mx), e1 = __expf(v1 - mx), n0 = __expf(mn - v0), n1 = __expf(mn - v1);
          float sp = e0 + e1, sn = n0 + n1;
#pragma unroll
          for (int o = 1; o < 16; o <<= 1) {
            sp += __shfl_xor(sp, o);
            sn += __shfl_xor(sn, o);
          }
          float ip = 1.f / sp, in_ = 1.f / sn;
          char* rowp = wl + (m * 16 + fq * 4 + r) * 144;
          *reinterpret_cast<u16*>(rowp + (fr)*2) = f2bf(e0 * ip);
          *reinterpret_cast<u16*>(rowp + (16 + fr) * 2) = f2bf(e1 * ip);
          *reinterpret_cast<u16*>(rowp + (32 + fr) * 2) = f2bf(n0 * in_);
          *reinterpret_cast<u16*>(rowp + (48 + fr) * 2) = f2bf(n1 * in_);
        }
      }
#pragma unroll
      for (int it = 0; it < 8; ++it) {
        int row = it * 8 + (lane >> 3), ch = lane & 7;
        bf16x8 v = *reinterpret_cast<const bf16x8*>(wl + row * 144 + ch * 16);
        *reinterpret_cast<bf16x8*>(phiq + (long)(grow0 + row) * 2048 + h * 64 + ch * 8) = v;
      }
    }
  } else if (ct < 24) {
    int h = 2 * (ct - 8) + wc;
#pragma unroll
    for (int m = 0; m < 4; ++m)
#pragma unroll
      for (int n = 0; n < 2; ++n)
#pragma unroll
        for (int r = 0; r < 4; ++r)
          *reinterpret_cast<u16*>(wl + (m * 16 + fq * 4 + r) * 80 + (n * 16 + fr) * 2) = f2bf(acc[m][n][r]);
    bf16x8 wb[2];
#pragma unroll
    for (int ft = 0; ft < 2; ++ft)
#pragma unroll
      for (int j = 0; j < 8; ++j) wb[ft][j] = (short)wphi[h * 1024 + (fq * 8 + j) * 32 + ft * 16 + fr];
    u16 ph[4][4][4];
    float zsum[4] = {0.f, 0.f, 0.f, 0.f};
#pragma unroll
    for (int m = 0; m < 4; ++m) {
      bf16x8 af = *reinterpret_cast<const bf16x8*>(wl + (m * 16 + fr) * 80 + (fq * 8) * 2);
      f32x4 p0 = {0.f, 0.f, 0.f, 0.f}, p1 = {0.f, 0.f, 0.f, 0.f};
      p0 = __builtin_amdgcn_mfma_f32_16x16x32_bf16(af, wb[0], p0, 0, 0, 0);
      p1 = __builtin_amdgcn_mfma_f32_16x16x32_bf16(af, wb[1], p1, 0, 0, 0);
#pragma unroll
      for (int r = 0; r < 4; ++r) {
        float v0 = rbf(p0[r]), v1 = rbf(p1[r]);
        float mx = fmaxf(v0, v1), mn = fminf(v0, v1);
#pragma unroll
        for (int o = 1; o < 16; o <<= 1) {
          mx = fmaxf(mx, __shfl_xor(mx, o));
          mn = fminf(mn, __shfl_xor(mn, o));
        }
        float e0 = __expf(v0 - mx), e1 = __expf(v1 - mx), n0 = __expf(mn - v0), n1 = __expf(mn - v1);
        float sp = e0 + e1, sn = n0 + n1;
#pragma unroll
        for (int o = 1; o < 16; o <<= 1) {
          sp += __shfl_xor(sp, o);
          sn += __shfl_xor(sn, o);
        }
        float ip = 1.f / sp, in_ = 1.f / sn;
        ph[m][0][r] = f2bf(e0 * ip);
        ph[m][1][r] = f2bf(e1 * ip);
        ph[m][2][r] = f2bf(n0 * in_);
        ph[m][3][r] = f2bf(n1 * in_);
#pragma unroll
        for (int ft = 0; ft < 4; ++ft) zsum[ft] += bf2f(ph[m][ft][r]);
      }
    }
    f32x4 kva[4][2];
#pragma unroll
    for (int ft = 0; ft < 4; ++ft)
#pragma unroll
      for (int dt = 0; dt < 2; ++dt) kva[ft][dt] = f32x4{0.f, 0.f, 0.f, 0.f};
#pragma unroll
    for (int mp = 0; mp < 2; ++mp) {
      bf16x8 vb[2];
#pragma unroll
      for (int dt = 0; dt < 2; ++dt)
#pragma unroll
        for (int j = 0; j < 8; ++j) vb[dt][j] = (short)f2bf(acc[mp * 2 + (j >> 2)][2 + dt][j & 3]);
#pragma unroll
      for (int ft = 0; ft < 4; ++ft) {
        bf16x8 pa;
#pragma unroll
        for (int j = 0; j < 8; ++j) pa[j] = (short)ph[mp * 2 + (j >> 2)][ft][j & 3];
#pragma unroll
        for (int dt = 0; dt < 2; ++dt)
          kva[ft][dt] = __builtin_amdgcn_mfma_f32_16x16x32_bf16(pa, vb[dt], kva[ft][dt], 0, 0, 0);
      }
    }
    int slab = rb * 2 + wr;
    float* kvp = kvs + ((long)slab * 32 + h) * 2048;
#pragma unroll
    for (int ft = 0; ft < 4; ++ft)
#pragma unroll
      for (int dt = 0; dt < 2; ++dt)
#pragma unroll
        for (int r = 0; r < 4; ++r) kvp[(ft * 16 + fq * 4 + r) * 32 + dt * 16 + fr] = kva[ft][dt][r];
#pragma unroll
    for (int ft = 0; ft < 4; ++ft) {
      float z = zsum[ft];
      z += __shfl_xor(z, 16);
      z += __shfl_xor(z, 32);
      if (fq == 0) zs[((long)slab * 32 + h) * 64 + ft * 16 + fr] = z;
    }
  } else {
    int c0 = (ct - 24) * 128 + wc * 64;
#pragma unroll
    for (int m = 0; m < 4; ++m)
#pragma unroll
      for (int r = 0; r < 4; ++r) {
        int row = m * 16 + fq * 4 + r;
        float mean = stx[(grow0 + row) * 2], rstd = stx[(grow0 + row) * 2 + 1];
#pragma unroll
        for (int n = 0; n < 4; ++n) {
          float v = rbf(rstd * (acc[m][n][r] - mean * cs1[c0 + n * 16 + fr]));
          *reinterpret_cast<u16*>(wl + row * 144 + (n * 16 + fr) * 2) = f2bf(gelu_t(v));
        }
      }
#pragma unroll
    for (int it = 0; it < 8; ++it) {
      int row = it * 8 + (lane >> 3), ch = lane & 7;
      bf16x8 v = *reinterpret_cast<const bf16x8*>(wl + row * 144 + ch * 16);
      *reinterpret_cast<bf16x8*>(hbuf + (long)(grow0 + row + 2) * D_ + c0 + ch * 8) = v;
    }
  }
}

__global__ __launch_bounds__(256) void k2_gemm(const u16* __restrict__ x, const u16* __restrict__ hbuf,
                                               const u16* __restrict__ w2, u16* __restrict__ x1,
                                               float* __restrict__ st2, const u16* __restrict__ phiq,
                                               const float* __restrict__ kvs, const float* __restrict__ zs,
                                               u16* __restrict__ obuf) {
  extern __shared__ __attribute__((aligned(16))) char smem[];
  constexpr int BM = 128, BN = 64, WM = 2, WN = 2, NW = 4;
  int b = blockIdx.x, tid = threadIdx.x, lane = tid & 63;
  int wid = __builtin_amdgcn_readfirstlane(tid >> 6);
  const int fr = lane & 15, fq = lane >> 4;
  if (b < 256) {
    int ct = (b & 7) * 2 + ((b >> 3) & 1), rb = b >> 4;
    const int wr = wid >> 1, wc = wid & 1;
    const u16* asrc[BM / 8 / NW];
    const u16* bsrc[BN / 8 / NW];
    make_asrc<BM, NW>(asrc, hbuf + (long)(rb * BM + 2) * D_, D_, wid, lane);
    make_bsrc<BN, NW>(bsrc, w2, D_, ct * BN, wid, lane);
    f32x4 acc[4][2];
#pragma unroll
    for (int m = 0; m < 4; ++m)
#pragma unroll
      for (int n = 0; n < 2; ++n) acc[m][n] = f32x4{0.f, 0.f, 0.f, 0.f};
    mainloop<BM, BN, WM, WN, true>(acc, asrc, D_, bsrc, D_, D_, smem, wid, lane);
    const int grow0 = rb * BM + wr * 64, c0 = ct * BN + wc * 32;
#pragma unroll
    for (int m = 0; m < 4; ++m)
#pragma unroll
      for (int r = 0; r < 4; ++r) {
        int row = grow0 + m * 16 + fq * 4 + r;
        float s = 0.f, ss = 0.f;
#pragma unroll
        for (int n = 0; n < 2; ++n) {
          long idx = (long)row * D_ + c0 + n * 16 + fr;
          float v = rbf(bf2f(x[idx]) + rbf(acc[m][n][r]));
          x1[idx] = f2bf(v);
          s += v;
          ss += v * v;
        }
#pragma unroll
        for (int o = 1; o < 16; o <<= 1) {
          s += __shfl_xor(s, o);
          ss += __shfl_xor(ss, o);
        }
        if (fr == 0) {
          int slot = ct * 2 + wc;
          st2[((long)row * 32 + slot) * 2] = s;
          st2[((long)row * 32 + slot) * 2 + 1] = ss;
        }
      }
  } else {
    int j = b - 256, h = j >> 1, half = j & 1;
    u16* kvb = reinterpret_cast<u16*>(smem);
    u16* zb = reinterpret_cast<u16*>(smem + 4096);
    for (int e = tid; e < 2048 + 64; e += 256) {
      float t = 0.f;
      if (e < 2048) {
        for (int sl = 0; sl < 32; ++sl) t += kvs[((long)sl * 32 + h) * 2048 + e];
        kvb[e] = f2bf(t);
      } else {
        for (int sl = 0; sl < 32; ++sl) t += zs[((long)sl * 32 + h) * 64 + (e - 2048)];
        zb[e - 2048] = f2bf(t);
      }
    }
    __syncthreads();
    bf16x8 kb[2][2], zf[2];
#pragma unroll
    for (int ks = 0; ks < 2; ++ks) {
#pragma unroll
      for (int jj = 0; jj < 8; ++jj) {
        kb[ks][0][jj] = (short)kvb[(ks * 32 + fq * 8 + jj) * 32 + fr];
        kb[ks][1][jj] = (short)kvb[(ks * 32 + fq * 8 + jj) * 32 + 16 + fr];
        zf[ks][jj] = (short)zb[ks * 32 + fq * 8 + jj];
      }
    }
    for (int it = 0; it < 16; ++it) {
      int row0 = half * 1024 + wid * 256 + it * 16;
      f32x4 n0 = {0.f, 0.f, 0.f, 0.f}, n1 = n0, dn = n0;
#pragma unroll
      for (int ks = 0; ks < 2; ++ks) {
        bf16x8 a = *reinterpret_cast<const bf16x8*>(phiq + (long)(row0 + fr) * 2048 + h * 64 + ks * 32 + fq * 8);
        n0 = __builtin_amdgcn_mfma_f32_16x16x32_bf16(a, kb[ks][0], n0, 0, 0, 0);
        n1 = __builtin_amdgcn_mfma_f32_16x16x32_bf16(a, kb[ks][1], n1, 0, 0, 0);
        dn = __builtin_amdgcn_mfma_f32_16x16x32_bf16(a, zf[ks], dn, 0, 0, 0);
      }
#pragma unroll
      for (int r = 0; r < 4; ++r) {
        float den = rbf(rbf(dn[r]) + 1e-6f);
        long idx = (long)(row0 + fq * 4 + r) * D_ + h * 32;
        obuf[idx + fr] = f2bf(rbf(n0[r]) / den);
        obuf[idx + 16 + fr] = f2bf(rbf(n1[r]) / den);
      }
    }
  }
}

__global__ __launch_bounds__(256) void k3_gemm(const u16* __restrict__ x1, const u16* __restrict__ w3,
                                               const float* __restrict__ st2, const float* __restrict__ cs3,
                                               u16* __restrict__ h3) {
  extern __shared__ __attribute__((aligned(16))) char smem[];
  constexpr int BM = 128, BN = 128, WM = 2, WN = 2, NW = 4;
  int b = blockIdx.x, tid = threadIdx.x, lane = tid & 63;
  int wid = __builtin_amdgcn_readfirstlane(tid >> 6);
  int ct = b % 28, rb = b / 28;
  const int wr = wid >> 1, wc = wid & 1, fr = lane & 15, fq = lane >> 4;
  float* lst = reinterpret_cast<float*>(smem + 65536);
  if (tid < 128) {
    float s = 0.f, ss = 0.f;
    const float* p = st2 + (long)(rb * BM + tid) * 64;
#pragma unroll
    for (int i = 0; i < 32; ++i) { s += p[2 * i]; ss += p[2 * i + 1]; }
    float m = s * (1.f / D_);
    float var = fmaxf(ss * (1.f / D_) - m * m, 0.f);
    lst[tid * 2] = m;
    lst[tid * 2 + 1] = rsqrtf(var + 1e-5f);
  }
  const u16* asrc[BM / 8 / NW];
  const u16* bsrc[BN / 8 / NW];
  make_asrc<BM, NW>(asrc, x1 + (long)rb * BM * D_, D_, wid, lane);
  make_bsrc<BN, NW>(bsrc, w3, F_, ct * BN, wid, lane);
  f32x4 acc[4][4];
#pragma unroll
  for (int m = 0; m < 4; ++m)
#pragma unroll
    for (int n = 0; n < 4; ++n) acc[m][n] = f32x4{0.f, 0.f, 0.f, 0.f};
  mainloop<BM, BN, WM, WN, false>(acc, asrc, D_, bsrc, F_, D_, smem, wid, lane);
  char* wl = smem + wid * 9216;
  const int grow0 = rb * BM + wr * 64, c0 = ct * BN + wc * 64;
#pragma unroll
  for (int m = 0; m < 4; ++m)
#pragma unroll
    for (int r = 0; r < 4; ++r) {
      int row = m * 16 + fq * 4 + r;
      float mean = lst[(wr * 64 + row) * 2], rstd = lst[(wr * 64 + row) * 2 + 1];
#pragma unroll
      for (int n = 0; n < 4; ++n) {
        float v = rbf(rstd * (acc[m][n][r] - mean * cs3[c0 + n * 16 + fr]));
        *reinterpret_cast<u16*>(wl + row * 144 + (n * 16 + fr) * 2) = f2bf(gelu_t(v));
      }
    }
#pragma unroll
  for (int it = 0; it < 8; ++it) {
    int row = it * 8 + (lane >> 3), ch = lane & 7;
    bf16x8 v = *reinterpret_cast<const bf16x8*>(wl + row * 144 + ch * 16);
    *reinterpret_cast<bf16x8*>(h3 + (long)(grow0 + row) * F_ + c0 + ch * 8) = v;
  }
}

__global__ __launch_bounds__(256) void k4_gemm(const u16* __restrict__ obuf, const u16* __restrict__ wo,
                                               const u16* __restrict__ h3, const u16* __restrict__ w4,
                                               const u16* __restrict__ x1, const float* __restrict__ gate,
                                               u16* __restrict__ out) {
  extern __shared__ __attribute__((aligned(16))) char smem[];
  constexpr int BM = 128, BN = 64, WM = 2, WN = 2, NW = 4;
  int b = blockIdx.x, tid = threadIdx.x, lane = tid & 63;
  int wid = __builtin_amdgcn_readfirstlane(tid >> 6);
  int ct = (b & 7) * 2 + ((b >> 3) & 1), rb = b >> 4;
  const int wr = wid >> 1, wc = wid & 1, fr = lane & 15, fq = lane >> 4;
  const u16* asrc[BM / 8 / NW];
  const u16* bsrc[BN / 8 / NW];
  f32x4 acca[4][2], acch[4][2];
#pragma unroll
  for (int m = 0; m < 4; ++m)
#pragma unroll
    for (int n = 0; n < 2; ++n) { acca[m][n] = f32x4{0.f, 0.f, 0.f, 0.f}; acch[m][n] = f32x4{0.f, 0.f, 0.f, 0.f}; }
  make_asrc<BM, NW>(asrc, obuf + (long)rb * BM * D_, D_, wid, lane);
  make_bsrc<BN, NW>(bsrc, wo, D_, ct * BN, wid, lane);
  mainloop<BM, BN, WM, WN, false>(acca, asrc, D_, bsrc, D_, D_, smem, wid, lane);
  make_asrc<BM, NW>(asrc, h3 + (long)rb * BM * F_, F_, wid, lane);
  make_bsrc<BN, NW>(bsrc, w4, D_, ct * BN, wid, lane);
  mainloop<BM, BN, WM, WN, false>(acch, asrc, F_, bsrc, D_, F_, smem, wid, lane);
  const int grow0 = rb * BM + wr * 64, c0 = ct * BN + wc * 32;
#pragma unroll
  for (int m = 0; m < 4; ++m)
#pragma unroll
    for (int r = 0; r < 4; ++r) {
      int row = grow0 + m * 16 + fq * 4 + r;
      float g = gate[row];
      float og = rbf(1.f - g);
#pragma unroll
      for (int n = 0; n < 2; ++n) {
        long idx = (long)row * D_ + c0 + n * 16 + fr;
        float a = rbf(acca[m][n][r]);
        float bb = rbf(bf2f(x1[idx]) + rbf(acch[m][n][r]));
        out[idx] = f2bf(rbf(g * a) + rbf(og * bb));
      }
    }
}

extern "C" void kernel_launch(void* const* d_in, const int* in_sizes, int n_in, void* d_out, int out_size,
                              void* d_ws, size_t ws_size, hipStream_t stream) {
  const u16* x = (const u16*)d_in[0];
  const u16* wqkv = (const u16*)d_in[1];
  const u16* wphi = (const u16*)d_in[2];
  const u16* wo = (const u16*)d_in[3];
  const u16* w1 = (const u16*)d_in[5];
  const u16* w2 = (const u16*)d_in[6];
  const u16* w3 = (const u16*)d_in[7];
  const u16* w4 = (const u16*)d_in[8];
  const u16* gw = (const u16*)d_in[9];
  char* ws = (char*)d_ws;
  u16* phiq = (u16*)(ws + OFF_PHIQ);
  float* kvs = (float*)(ws + OFF_KVS);
  float* zs = (float*)(ws + OFF_ZS);
  u16* hbuf = (u16*)(ws + OFF_H);
  u16* x1 = (u16*)(ws + OFF_X1);
  float* st2 = (float*)(ws + OFF_ST2);
  u16* obuf = (u16*)(ws + OFF_O);
  u16* h3 = (u16*)(ws + OFF_H3);
  float* gate = (float*)(ws + OFF_GATE);
  float* stx = (float*)(ws + OFF_STX);
  float* cs1 = (float*)(ws + OFF_CS1);
  float* cs3 = (float*)(ws + OFF_CS3);
  (void)hipFuncSetAttribute((const void*)k3_gemm, hipFuncAttributeMaxDynamicSharedMemorySize, 65536 + 1024);
  k0_stats<<<549, 256, 0, stream>>>(x, gw, w1, w3, stx, gate, cs1, cs3, hbuf);
  k1_gemm<<<512, 256, 65536, stream>>>(x, wqkv, wphi, w1, stx, cs1, phiq, kvs, zs, hbuf);
  k2_gemm<<<256 + 64, 256, 49152, stream>>>(x, hbuf, w2, x1, st2, phiq, kvs, zs, obuf);
  k3_gemm<<<448, 256, 65536 + 1024, stream>>>(x1, w3, st2, cs3, h3);
  k4_gemm<<<256, 256, 49152, stream>>>(obuf, wo, h3, w4, x1, gate, (u16*)d_out);
}
```

```cpp
#include <hip/hip_runtime.h>
#include <hip/hip_bf16.h>
#include <stdint.h>

typedef __attribute__((ext_vector_type(8))) short bf16x8;
typedef __attribute__((ext_vector_type(4))) short bf16x4;
typedef __attribute__((ext_vector_type(4))) float f32x4;
typedef unsigned short u16;
#define LDS_AS __attribute__((address_space(3)))

constexpr int S_ = 2048, D_ = 1024, F_ = 3584;

__device__ __forceinline__ float bf2f(u16 u) { return __uint_as_float(((unsigned)u) << 16); }
__device__ __forceinline__ u16 f2bf(float f) {
  __hip_bfloat16 b = __float2bfloat16(f);
  return *reinterpret_cast<u16*>(&b);
}
__device__ __forceinline__ float rbf(float f) { return bf2f(f2bf(f)); }
__device__ __forceinline__ float gelu_t(float x) {
  float u = 0.7978845608028654f * (x + 0.044715f * x * x * x);
  float e = __expf(2.f * u);
  float t = 1.f - 2.f / (1.f + e);
  return 0.5f * x * (1.f + t);
}

constexpr size_t OFF_PHIQ = 0;
constexpr size_t OFF_KVS = OFF_PHIQ + (size_t)2048 * 2048 * 2;
constexpr size_t OFF_ZS = OFF_KVS + (size_t)32 * 32 * 2048 * 4;
constexpr size_t OFF_H = OFF_ZS + (size_t)32 * 32 * 64 * 4;
constexpr size_t OFF_X1 = OFF_H + (size_t)2052 * 1024 * 2;
constexpr size_t OFF_ST2 = OFF_X1 + (size_t)2048 * 1024 * 2;
constexpr size_t OFF_O = OFF_ST2 + (size_t)2048 * 64 * 4;
constexpr size_t OFF_H3 = OFF_O + (size_t)2048 * 1024 * 2;
constexpr size_t OFF_GATE = OFF_H3 + (size_t)2048 * 3584 * 2;
constexpr size_t OFF_CS3 = OFF_GATE + 2048 * 4;


__device__ __forceinline__ void glds16(const void* gsrc, unsigned lds_dst) {
  unsigned keep;
  asm volatile("s_mov_b32 %0, m0\n\ts_mov_b32 m0, %2\n\ts_nop 0\n\tglobal_load_lds_dwordx4 %1, off\n\ts_mov_b32 m0, %0"
               : "=&s"(keep) : "v"(gsrc), "s"(lds_dst) : "memory");
}

template <int BN>
__device__ __forceinline__ int bswz(int krow) {
  if constexpr (BN == 128) return ((krow & 3) | (((krow >> 3) & 1) << 2)) << 1;
  else return (((krow >> 1) & 1) | (((krow >> 3) & 1) << 1)) << 1;
}

template <int BM, int BN, int WM, int WN, int NS, bool SHIFT, bool STATS>
__device__ __forceinline__ void mainloop(f32x4 (&acc)[BM / WM / 16][BN / WN / 16],
                                         const u16* const (&asrc)[BM / 8 / (WM * WN)], int lda,
                                         const u16* const (&bsrc)[BN / 8 / (WM * WN)], int ldb, int K,
                                         char* smem, int wid, int lane, float& st_s, float& st_ss, float (&st_c)[8]) {
  constexpr int NW = WM * WN, TM = BM / WM / 16, TN = BN / WN / 16;
  constexpr int NAI = BM / 8 / NW, NBI = BN / 8 / NW, NLD = NAI + NBI;
  constexpr int ABYTES = BM * 128, BBYTES = 64 * BN * 2, STAGE = ABYTES + BBYTES;
  constexpr int BROW = BN * 2;
  const int wr = wid / WN, wc = wid % WN, fr = lane & 15, fq = lane >> 4;
  const int nt = K / 64;
  auto stage = [&](int t) {
    unsigned la = (unsigned)(size_t)(LDS_AS char*)smem + (unsigned)((t % NS) * STAGE);
    unsigned lb = la + ABYTES;
    long aoff = (long)t * 64;
    if constexpr (SHIFT) {
      int g = t >> 2;
      int dl = (g == 0) ? -1 : (g == 1) ? 1 : (g == 2) ? -2 : 2;
      aoff += (long)dl * lda;
    }
#pragma unroll
    for (int i = 0; i < NAI; ++i)
      glds16(asrc[i] + aoff, la + (wid * NAI + i) * 1024);
    long boff = (long)t * 64 * ldb;
#pragma unroll
    for (int i = 0; i < NBI; ++i)
      glds16(bsrc[i] + boff, lb + (wid * NBI + i) * 1024);
  };
  int aofs[2], bofs[2][TN][2];
#pragma unroll
  for (int ks = 0; ks < 2; ++ks) {
    int row = wr * (TM * 16) + fr;
    aofs[ks] = row * 128 + (((ks * 4 + fq) ^ (row & 7)) << 4);
#pragma unroll
    for (int n = 0; n < TN; ++n)
#pragma unroll
      for (int hh = 0; hh < 2; ++hh) {
        int col = wc * (TN * 16) + n * 16 + (fr & 3) * 4;
        int krow = ks * 32 + fq * 8 + hh * 4 + (fr >> 2);
        bofs[ks][n][hh] = krow * BROW + (((col >> 3) ^ bswz<BN>(krow)) << 4) + ((col & 4) << 1);
      }
  }
#pragma unroll
  for (int t = 0; t < NS - 1; ++t) stage(t);
  for (int t = 0; t < nt; ++t) {
    if (t + NS - 1 < nt + 0 && t + NS - 2 < nt - 0 && t <= nt - NS) {
      if constexpr (NS == 3) asm volatile("s_waitcnt vmcnt(%0)" ::"i"(NLD) : "memory");
      else if constexpr (NS == 4) asm volatile("s_waitcnt vmcnt(%0)" ::"i"(2 * NLD) : "memory");
      else asm volatile("s_waitcnt vmcnt(0)" ::: "memory");
    } else {
      asm volatile("s_waitcnt vmcnt(0)" ::: "memory");
    }
    asm volatile("s_waitcnt lgkmcnt(0)" ::: "memory");
    __builtin_amdgcn_s_barrier();
    if (t + NS - 1 < nt) stage(t + NS - 1);
    const char* la = smem + (t % NS) * STAGE;
    const char* lb = la + ABYTES;
    if constexpr (STATS) {
      const int tid = wid * 64 + lane;
      constexpr int NT = NW * 64;
      constexpr int TPR = NT / BM;
      constexpr int CPT = 8 / TPR;
      int row = tid / TPR, hf = tid % TPR;
#pragma unroll
      for (int c = 0; c < CPT; ++c) {
        uint4 v = *reinterpret_cast<const uint4*>(la + row * 128 + (hf * CPT + c) * 16);
        unsigned w[4] = {v.x, v.y, v.z, v.w};
#pragma unroll
        for (int q = 0; q < 4; ++q) {
          float lo = __uint_as_float(w[q] << 16), hi = __uint_as_float(w[q] & 0xffff0000u);
          st_s += lo + hi;
          st_ss = fmaf(lo, lo, fmaf(hi, hi, st_ss));
        }
      }
      constexpr int LPR = BN / 8, RPT = 64 / (NT / LPR);
      int ch = tid % LPR, kr0 = (tid / LPR) * RPT;
#pragma unroll
      for (int i = 0; i < RPT; ++i) {
        int krow = kr0 + i;
        bf16x8 v = *reinterpret_cast<const bf16x8*>(lb + krow * BROW + ((ch ^ bswz<BN>(krow)) << 4));
#pragma unroll
        for (int j = 0; j < 8; ++j) st_c[j] += bf2f((u16)v[j]);
      }
    }
#pragma unroll
    for (int ks = 0; ks < 2; ++ks) {
      bf16x8 af[TM];
      bf16x8 bfr[TN];
#pragma unroll
      for (int m = 0; m < TM; ++m) af[m] = *reinterpret_cast<const bf16x8*>(la + (aofs[ks] + m * 2048));
#pragma unroll
      for (int n = 0; n < TN; ++n) {
        bf16x4 lo = __builtin_amdgcn_ds_read_tr16_b64_v4i16((LDS_AS bf16x4*)(lb + bofs[ks][n][0]));
        bf16x4 hi = __builtin_amdgcn_ds_read_tr16_b64_v4i16((LDS_AS bf16x4*)(lb + bofs[ks][n][1]));
        bfr[n] = __builtin_shufflevector(lo, hi, 0, 1, 2, 3, 4, 5, 6, 7);
      }
#pragma unroll
      for (int m = 0; m < TM; ++m)
#pragma unroll
        for (int n = 0; n < TN; ++n)
          acc[m][n] = __builtin_amdgcn_mfma_f32_16x16x32_bf16(af[m], bfr[n], acc[m][n], 0, 0, 0);
    }
  }
  asm volatile("s_waitcnt lgkmcnt(0)" ::: "memory");
  __builtin_amdgcn_s_barrier();
}

template <int BM, int NW>
__device__ __forceinline__ void make_asrc(const u16* (&asrc)[BM / 8 / NW], const u16* Ablk, int lda, int wid, int lane) {
#pragma unroll
  for (int i = 0; i < BM / 8 / NW; ++i) {
    int row = 8 * (wid * (BM / 8 / NW) + i) + (lane >> 3);
    int ch = (lane & 7) ^ (row & 7);
    asrc[i] = Ablk + (long)row * lda + ch * 8;
  }
}
template <int BN, int NW>
__device__ __forceinline__ void make_bsrc(const u16* (&bsrc)[BN / 8 / NW], const u16* W, int ldb, int colbase, int wid, int lane) {
  constexpr int LPR = BN / 8;
#pragma unroll
  for (int i = 0; i < BN / 8 / NW; ++i) {
    int krow = (64 / LPR) * (wid * (BN / 8 / NW) + i) + lane / LPR;
    int ch = (lane % LPR) ^ bswz<BN>(krow);
    bsrc[i] = W + (long)krow * ldb + colbase + ch * 8;
  }
}

__device__ __forceinline__ void hedgehog4(const f32x4& p0, const f32x4& p1, u16 (&o)[4][4]) {
#pragma unroll
  for (int r = 0; r < 4; ++r) {
    float v0 = rbf(p0[r]), v1 = rbf(p1[r]);
    float mx = fmaxf(v0, v1), mn = fminf(v0, v1);
#pragma unroll
    for (int of = 1; of < 16; of <<= 1) {
      mx = fmaxf(mx, __shfl_xor(mx, of));
      mn = fminf(mn, __shfl_xor(mn, of));
    }
    float e0 = __expf(v0 - mx), e1 = __expf(v1 - mx), n0 = __expf(mn - v0), n1 = __expf(mn - v1);
    float sp = e0 + e1, sn = n0 + n1;
#pragma unroll
    for (int of = 1; of < 16; of <<= 1) {
      sp += __shfl_xor(sp, of);
      sn += __shfl_xor(sn, of);
    }
    float ip = 1.f / sp, in_ = 1.f / sn;
    o[0][r] = f2bf(e0 * ip);
    o[1][r] = f2bf(e1 * ip);
    o[2][r] = f2bf(n0 * in_);
    o[3][r] = f2bf(n1 * in_);
  }
}

constexpr int K1_LDS = 3 * (256 * 128 + 64 * 128 * 2);
__global__ __launch_bounds__(512) void k1_gemm(const u16* __restrict__ x, const u16* __restrict__ wqkv,
                                               const u16* __restrict__ wphi, const u16* __restrict__ w1,
                                               const u16* __restrict__ w3, const u16* __restrict__ gw,
                                               u16* __restrict__ phiq, float* __restrict__ kvs, float* __restrict__ zs,
                                               u16* __restrict__ hbuf, float* __restrict__ gate, float* __restrict__ cs3) {
  extern __shared__ __attribute__((aligned(16))) char smem[];
  constexpr int BM = 256, BN = 128, WM = 4, WN = 2, NW = 8, NS = 3;
  int b = blockIdx.x, tid = threadIdx.x, lane = tid & 63;
  int wid = __builtin_amdgcn_readfirstlane(tid >> 6);
  const int fr = lane & 15, fq = lane >> 4;
  if (b >= 256) {
    int e = b - 256;
    if (e < 32) {
      for (int i = 0; i < 8; ++i) {
        int row = e * 64 + wid * 8 + i;
        const u16* xr = x + (long)row * D_;
        float gs = 0.f;
#pragma unroll
        for (int c = 0; c < 2; ++c) {
          bf16x8 v = *reinterpret_cast<const bf16x8*>(xr + c * 512 + lane * 8);
          bf16x8 g = *reinterpret_cast<const bf16x8*>(gw + c * 512 + lane * 8);
#pragma unroll
          for (int j = 0; j < 8; ++j) gs += rbf(bf2f((u16)v[j]) * bf2f((u16)g[j]));
        }
#pragma unroll
        for (int o = 1; o < 64; o <<= 1) gs += __shfl_xor(gs, o);
        if (lane == 0) gate[row] = rbf(1.f / (1.f + __expf(-rbf(gs))));
      }
    } else if (e < 32 + 28) {
      float* red = reinterpret_cast<float*>(smem);
      int c0 = (e - 32) * 128;
      int ksl = tid >> 4, ch = tid & 15;
      float a[8] = {0, 0, 0, 0, 0, 0, 0, 0};
      for (int r = 0; r < 32; ++r) {
        bf16x8 v = *reinterpret_cast<const bf16x8*>(w3 + (long)(ksl * 32 + r) * F_ + c0 + ch * 8);
#pragma unroll
        for (int j = 0; j < 8; ++j) a[j] += bf2f((u16)v[j]);
      }
#pragma unroll
      for (int j = 0; j < 8; ++j) red[ksl * 128 + ch * 8 + j] = a[j];
      __syncthreads();
      if (tid < 128) {
        float t = 0.f;
#pragma unroll
        for (int i = 0; i < 32; ++i) t += red[i * 128 + tid];
        cs3[c0 + tid] = t;
      }
    } else {
      for (int i = tid; i < 4 * D_ / 8; i += 512) {
        int r = i / (D_ / 8), c = i % (D_ / 8);
        int rr = (r < 2) ? r : 2048 + r;
        bf16x8 z = {0, 0, 0, 0, 0, 0, 0, 0};
        *reinterpret_cast<bf16x8*>(hbuf + (long)rr * D_ + c * 8) = z;
      }
    }
    return;
  }
  int ct = (b & 3) * 8 + (b >> 5), rb = ((b >> 2) & 1) * 4 + ((b >> 3) & 3);
  const int wr = wid >> 1, wc = wid & 1;
  const u16* asrc[BM / 8 / NW];
  const u16* bsrc[BN / 8 / NW];
  make_asrc<BM, NW>(asrc, x + (long)rb * BM * D_, D_, wid, lane);
  f32x4 acc[4][4];
#pragma unroll
  for (int m = 0; m < 4; ++m)
#pragma unroll
    for (int n = 0; n < 4; ++n) acc[m][n] = f32x4{0.f, 0.f, 0.f, 0.f};
  float st_s = 0.f, st_ss = 0.f, st_c[8] = {0, 0, 0, 0, 0, 0, 0, 0};
  char* wl = smem + wid * 9216;
  const int grow0 = rb * BM + wr * 64;
  if (ct < 24) {
    if (ct < 8) {
      make_bsrc<BN, NW>(bsrc, wqkv, 3 * D_, ct * 128, wid, lane);
    } else {
      int hp = ct - 8;
#pragma unroll
      for (int i = 0; i < BN / 8 / NW; ++i) {
        int krow = 4 * (wid * (BN / 8 / NW) + i) + (lane >> 4);
        int ch = (lane & 15) ^ bswz<BN>(krow);
        int col = ch * 8;
        int gcol = (((col >> 5) & 1) ? 2048 : 1024) + (2 * hp + (col >> 6)) * 32 + (col & 31);
        bsrc[i] = wqkv + (long)krow * (3 * D_) + gcol;
      }
    }
    mainloop<BM, BN, WM, WN, NS, false, false>(acc, asrc, D_, bsrc, 3 * D_, D_, smem, wid, lane, st_s, st_ss, st_c);
    if (ct < 8) {
#pragma unroll
      for (int m = 0; m < 4; ++m)
#pragma unroll
        for (int n = 0; n < 4; ++n)
#pragma unroll
          for (int r = 0; r < 4; ++r)
            *reinterpret_cast<u16*>(wl + (m * 16 + fq * 4 + r) * 144 + (n * 16 + fr) * 2) = f2bf(acc[m][n][r]);
      bf16x8 af[2][4];
#pragma unroll
      for (int hh = 0; hh < 2; ++hh)
#pragma unroll
        for (int m = 0; m < 4; ++m)
          af[hh][m] = *reinterpret_cast<const bf16x8*>(wl + (m * 16 + fr) * 144 + (hh * 32 + fq * 8) * 2);
#pragma unroll
      for (int hh = 0; hh < 2; ++hh) {
        int h = ct * 4 + wc * 2 + hh;
        bf16x8 wb[2];
#pragma unroll
        for (int ft = 0; ft < 2; ++ft)
#pragma unroll
          for (int j = 0; j < 8; ++j) wb[ft][j] = (short)wphi[h * 1024 + (fq * 8 + j) * 32 + ft * 16 + fr];
#pragma unroll
        for (int m = 0; m < 4; ++m) {
          f32x4 p0 = {0.f, 0.f, 0.f, 0.f}, p1 = {0.f, 0.f, 0.f, 0.f};
          p0 = __builtin_amdgcn_mfma_f32_16x16x32_bf16(af[hh][m], wb[0], p0, 0, 0, 0);
          p1 = __builtin_amdgcn_mfma_f32_16x16x32_bf16(af[hh][m], wb[1], p1, 0, 0, 0);
          u16 o[4][4];
          hedgehog4(p0, p1, o);
#pragma unroll
          for (int r = 0; r < 4; ++r) {
            char* rowp = wl + (m * 16 + fq * 4 + r) * 144;
#pragma unroll
            for (int ft = 0; ft < 4; ++ft) *reinterpret_cast<u16*>(rowp + (ft * 16 + fr) * 2) = o[ft][r];
          }
        }
#pragma unroll
        for (int it = 0; it < 8; ++it) {
          int row = it * 8 + (lane >> 3), ch = lane & 7;
          bf16x8 v = *reinterpret_cast<const bf16x8*>(wl + row * 144 + ch * 16);
          *reinterpret_cast<bf16x8*>(phiq + (long)(grow0 + row) * 2048 + h * 64 + ch * 8) = v;
        }
      }
    } else {
      int h = 2 * (ct - 8) + wc;
#pragma unroll
      for (int m = 0; m < 4; ++m)
#pragma unroll
        for (int n = 0; n < 2; ++n)
#pragma unroll
          for (int r = 0; r < 4; ++r)
            *reinterpret_cast<u16*>(wl + (m * 16 + fq * 4 + r) * 80 + (n * 16 + fr) * 2) = f2bf(acc[m][n][r]);
      bf16x8 wb[2];
#pragma unroll
      for (int ft = 0; ft < 2; ++ft)
#pragma unroll
        for (int j = 0; j < 8; ++j) wb[ft][j] = (short)wphi[h * 1024 + (fq * 8 + j) * 32 + ft * 16 + fr];
      u16 ph[4][4][4];
      float zsum[4] = {0.f, 0.f, 0.f, 0.f};
#pragma unroll
      for (int m = 0; m < 4; ++m) {
        bf16x8 af = *reinterpret_cast<const bf16x8*>(wl + (m * 16 + fr) * 80 + (fq * 8) * 2);
        f32x4 p0 = {0.f, 0.f, 0.f, 0.f}, p1 = {0.f, 0.f, 0.f, 0.f};
        p0 = __builtin_amdgcn_mfma_f32_16x16x32_bf16(af, wb[0], p0, 0, 0, 0);
        p1 = __builtin_amdgcn_mfma_f32_16x16x32_bf16(af, wb[1], p1, 0, 0, 0);
        hedgehog4(p0, p1, ph[m]);
#pragma unroll
        for (int ft = 0; ft < 4; ++ft)
#pragma unroll
          for (int r = 0; r < 4; ++r) zsum[ft] += bf2f(ph[m][ft][r]);
      }
      f32x4 kva[4][2];
#pragma unroll
      for (int ft = 0; ft < 4; ++ft)
#pragma unroll
        for (int dt = 0; dt < 2; ++dt) kva[ft][dt] = f32x4{0.f, 0.f, 0.f, 0.f};
#pragma unroll
      for (int mp = 0; mp < 2; ++mp) {
        bf16x8 vb[2];
#pragma unroll
        for (int dt = 0; dt < 2; ++dt)
#pragma unroll
          for (int j = 0; j < 8; ++j) vb[dt][j] = (short)f2bf(acc[mp * 2 + (j >> 2)][2 + dt][j & 3]);
#pragma unroll
        for (int ft = 0; ft < 4; ++ft) {
          bf16x8 pa;
#pragma unroll
          for (int j = 0; j < 8; ++j) pa[j] = (short)ph[mp * 2 + (j >> 2)][ft][j & 3];
#pragma unroll
          for (int dt = 0; dt < 2; ++dt)
            kva[ft][dt] = __builtin_amdgcn_mfma_f32_16x16x32_bf16(pa, vb[dt], kva[ft][dt], 0, 0, 0);
        }
      }
      int slab = rb * 4 + wr;
      float* kvp = kvs + ((long)slab * 32 + h) * 2048;
#pragma unroll
      for (int ft = 0; ft < 4; ++ft)
#pragma unroll
        for (int dt = 0; dt < 2; ++dt)
#pragma unroll
          for (int r = 0; r < 4; ++r) kvp[(ft * 16 + fq * 4 + r) * 32 + dt * 16 + fr] = kva[ft][dt][r];
#pragma unroll
      for (int ft = 0; ft < 4; ++ft) {
        float z = zsum[ft];
        z += __shfl_xor(z, 16);
        z += __shfl_xor(z, 32);
        if (fq == 0) zs[((long)slab * 32 + h) * 64 + ft * 16 + fr] = z;
      }
    }
  } else {
    make_bsrc<BN, NW>(bsrc, w1, D_, (ct - 24) * 128, wid, lane);
    mainloop<BM, BN, WM, WN, NS, false, true>(acc, asrc, D_, bsrc, D_, D_, smem, wid, lane, st_s, st_ss, st_c);
    float* red = reinterpret_cast<float*>(smem + 81920);
    float* rst = reinterpret_cast<float*>(smem + 98304);
    float* csl = reinterpret_cast<float*>(smem + 100352);
    {
      float s = st_s + __shfl_xor(st_s, 1), ss = st_ss + __shfl_xor(st_ss, 1);
      if ((tid & 1) == 0) {
        float m = s * (1.f / D_);
        float var = fmaxf(ss * (1.f / D_) - m * m, 0.f);
        rst[(tid >> 1) * 2] = m;
        rst[(tid >> 1) * 2 + 1] = rsqrtf(var + 1e-5f);
      }
#pragma unroll
      for (int j = 0; j < 8; ++j) red[(tid >> 4) * 128 + (tid & 15) * 8 + j] = st_c[j];
    }
    __syncthreads();
    if (tid < 128) {
      float t = 0.f;
#pragma unroll
      for (int i = 0; i < 32; ++i) t += red[i * 128 + tid];
      csl[tid] = t;
    }
    __syncthreads();
    int c0 = (ct - 24) * 128 + wc * 64;
#pragma unroll
    for (int m = 0; m < 4; ++m)
#pragma unroll
      for (int r = 0; r < 4; ++r) {
        int row = m * 16 + fq * 4 + r;
        float mean = rst[(wr * 64 + row) * 2], rstd = rst[(wr * 64 + row) * 2 + 1];
#pragma unroll
        for (int n = 0; n < 4; ++n) {
          float v = rbf(rstd * (acc[m][n][r] - mean * csl[wc * 64 + n * 16 + fr]));
          *reinterpret_cast<u16*>(wl + row * 144 + (n * 16 + fr) * 2) = f2bf(gelu_t(v));
        }
      }
#pragma unroll
    for (int it = 0; it < 8; ++it) {
      int row = it * 8 + (lane >> 3), ch = lane & 7;
      bf16x8 v = *reinterpret_cast<const bf16x8*>(wl + row * 144 + ch * 16);
      *reinterpret_cast<bf16x8*>(hbuf + (long)(grow0 + row + 2) * D_ + c0 + ch * 8) = v;
    }
  }
}

constexpr int K2_LDS = 3 * (128 * 128 + 64 * 64 * 2);
__global__ __launch_bounds__(256) void k2_gemm(const u16* __restrict__ x, const u16* __restrict__ hbuf,
                                               const u16* __restrict__ w2, u16* __restrict__ x1,
                                               float* __restrict__ st2, const u16* __restrict__ phiq,
                                               const float* __restrict__ kvs, const float* __restrict__ zs,
                                               u16* __restrict__ obuf) {
  extern __shared__ __attribute__((aligned(16))) char smem[];
  constexpr int BM = 128, BN = 64, WM = 2, WN = 2, NW = 4, NS = 3;
  int b = blockIdx.x, tid = threadIdx.x, lane = tid & 63;
  int wid = __builtin_amdgcn_readfirstlane(tid >> 6);
  const int fr = lane & 15, fq = lane >> 4;
  if (b < 256) {
    int ct = (b & 1) * 8 + (b >> 5), rb = ((b >> 1) & 3) * 4 + ((b >> 3) & 3);
    const int wr = wid >> 1, wc = wid & 1;
    const u16* asrc[BM / 8 / NW];
    const u16* bsrc[BN / 8 / NW];
    make_asrc<BM, NW>(asrc, hbuf + (long)(rb * BM + 2) * D_, D_, wid, lane);
    make_bsrc<BN, NW>(bsrc, w2, D_, ct * BN, wid, lane);
    f32x4 acc[4][2];
#pragma unroll
    for (int m = 0; m < 4; ++m)
#pragma unroll
      for (int n = 0; n < 2; ++n) acc[m][n] = f32x4{0.f, 0.f, 0.f, 0.f};
    float st_s = 0.f, st_ss = 0.f, st_c[8] = {0, 0, 0, 0, 0, 0, 0, 0};
    mainloop<BM, BN, WM, WN, NS, true, false>(acc, asrc, D_, bsrc, D_, D_, smem, wid, lane, st_s, st_ss, st_c);
    const int grow0 = rb * BM + wr * 64, c0 = ct * BN + wc * 32;
    char* wl = smem + wid * 5120;
#pragma unroll
    for (int m = 0; m < 4; ++m)
#pragma unroll
      for (int n = 0; n < 2; ++n)
#pragma unroll
        for (int r = 0; r < 4; ++r)
          *reinterpret_cast<u16*>(wl + (m * 16 + fq * 4 + r) * 80 + (n * 16 + fr) * 2) = f2bf(acc[m][n][r]);
#pragma unroll
    for (int it = 0; it < 4; ++it) {
      int row = it * 16 + (lane >> 2), ch = lane & 3;
      bf16x8 hs = *reinterpret_cast<const bf16x8*>(wl + row * 80 + ch * 16);
      long idx = (long)(grow0 + row) * D_ + c0 + ch * 8;
      bf16x8 xv = *reinterpret_cast<const bf16x8*>(x + idx);
      bf16x8 o;
      float s = 0.f, ss = 0.f;
#pragma unroll
      for (int j = 0; j < 8; ++j) {
        float v = rbf(bf2f((u16)xv[j]) + bf2f((u16)hs[j]));
        o[j] = (short)f2bf(v);
        s += v;
        ss += v * v;
      }
      *reinterpret_cast<bf16x8*>(x1 + idx) = o;
      s += __shfl_xor(s, 1); ss += __shfl_xor(ss, 1);
      s += __shfl_xor(s, 2); ss += __shfl_xor(ss, 2);
      if (ch == 0) {
        int slot = ct * 2 + wc;
        st2[((long)(grow0 + row) * 32 + slot) * 2] = s;
        st2[((long)(grow0 + row) * 32 + slot) * 2 + 1] = ss;
      }
    }
  } else {
    int j = b - 256, h = j >> 1, half = j & 1;
    u16* kvb = reinterpret_cast<u16*>(smem);
    u16* zb = reinterpret_cast<u16*>(smem + 4096);
    for (int e = tid; e < 2048 + 64; e += 256) {
      float t = 0.f;
      if (e < 2048) {
        for (int sl = 0; sl < 32; ++sl) t += kvs[((long)sl * 32 + h) * 2048 + e];
        kvb[e] = f2bf(t);
      } else {
        for (int sl = 0; sl < 32; ++sl) t += zs[((long)sl * 32 + h) * 64 + (e - 2048)];
        zb[e - 2048] = f2bf(t);
      }
    }
    __syncthreads();
    bf16x8 kb[2][2], zf[2];
#pragma unroll
    for (int ks = 0; ks < 2; ++ks) {
#pragma unroll
      for (int jj = 0; jj < 8; ++jj) {
        kb[ks][0][jj] = (short)kvb[(ks * 32 + fq * 8 + jj) * 32 + fr];
        kb[ks][1][jj] = (short)kvb[(ks * 32 + fq * 8 + jj) * 32 + 16 + fr];
        zf[ks][jj] = (short)zb[ks * 32 + fq * 8 + jj];
      }
    }
    for (int it = 0; it < 16; ++it) {
      int row0 = half * 1024 + wid * 256 + it * 16;
      f32x4 n0 = {0.f, 0.f, 0.f, 0.f}, n1 = n0, dn = n0;
#pragma unroll
      for (int ks = 0; ks < 2; ++ks) {
        bf16x8 a = *reinterpret_cast<const bf16x8*>(phiq + (long)(row0 + fr) * 2048 + h * 64 + ks * 32 + fq * 8);
        n0 = __builtin_amdgcn_mfma_f32_16x16x32_bf16(a, kb[ks][0], n0, 0, 0, 0);
        n1 = __builtin_amdgcn_mfma_f32_16x16x32_bf16(a, kb[ks][1], n1, 0, 0, 0);
        dn = __builtin_amdgcn_mfma_f32_16x16x32_bf16(a, zf[ks], dn, 0, 0, 0);
      }
#pragma unroll
      for (int r = 0; r < 4; ++r) {
        float den = rbf(rbf(dn[r]) + 1e-6f);
        long idx = (long)(row0 + fq * 4 + r) * D_ + h * 32;
        obuf[idx + fr] = f2bf(rbf(n0[r]) / den);
        obuf[idx + 16 + fr] = f2bf(rbf(n1[r]) / den);
      }
    }
  }
}

constexpr int K3_LDS = K1_LDS + 2048;
__global__ __launch_bounds__(512) void k3_gemm(const u16* __restrict__ x1, const u16* __restrict__ w3,
                                               const float* __restrict__ st2, const float* __restrict__ cs3,
                                               u16* __restrict__ h3) {
  extern __shared__ __attribute__((aligned(16))) char smem[];
  constexpr int BM = 256, BN = 128, WM = 4, WN = 2, NW = 8, NS = 3;
  int b = blockIdx.x, tid = threadIdx.x, lane = tid & 63;
  int wid = __builtin_amdgcn_readfirstlane(tid >> 6);
  int ct = (b & 3) * 7 + (b >> 5), rb = ((b >> 2) & 1) * 4 + ((b >> 3) & 3);
  const int wr = wid >> 1, wc = wid & 1, fr = lane & 15, fq = lane >> 4;
  float* lst = reinterpret_cast<float*>(smem + K1_LDS);
  if (tid < 256) {
    float s = 0.f, ss = 0.f;
    const float4* p = reinterpret_cast<const float4*>(st2 + (long)(rb * BM + tid) * 64);
#pragma unroll
    for (int i = 0; i < 16; ++i) { float4 v = p[i]; s += v.x + v.z; ss += v.y + v.w; }
    float m = s * (1.f / D_);
    float var = fmaxf(ss * (1.f / D_) - m * m, 0.f);
    lst[tid * 2] = m;
    lst[tid * 2 + 1] = rsqrtf(var + 1e-5f);
  }
  const u16* asrc[BM / 8 / NW];
  const u16* bsrc[BN / 8 / NW];
  make_asrc<BM, NW>(asrc, x1 + (long)rb * BM * D_, D_, wid, lane);
  make_bsrc<BN, NW>(bsrc, w3, F_, ct * BN, wid, lane);
  f32x4 acc[4][4];
#pragma unroll
  for (int m = 0; m < 4; ++m)
#pragma unroll
    for (int n = 0; n < 4; ++n) acc[m][n] = f32x4{0.f, 0.f, 0.f, 0.f};
  float st_s = 0.f, st_ss = 0.f, st_c[8] = {0, 0, 0, 0, 0, 0, 0, 0};
  mainloop<BM, BN, WM, WN, NS, false, false>(acc, asrc, D_, bsrc, F_, D_, smem, wid, lane, st_s, st_ss, st_c);
  char* wl = smem + wid * 9216;
  const int grow0 = rb * BM + wr * 64, c0 = ct * BN + wc * 64;
#pragma unroll
  for (int m = 0; m < 4; ++m)
#pragma unroll
    for (int r = 0; r < 4; ++r) {
      int row = m * 16 + fq * 4 + r;
      float mean = lst[(wr * 64 + row) * 2], rstd = lst[(wr * 64 + row) * 2 + 1];
#pragma unroll
      for (int n = 0; n < 4; ++n) {
        float v = rbf(rstd * (acc[m][n][r] - mean * cs3[c0 + n * 16 + fr]));
        *reinterpret_cast<u16*>(wl + row * 144 + (n * 16 + fr) * 2) = f2bf(gelu_t(v));
      }
    }
#pragma unroll
  for (int it = 0; it < 8; ++it) {
    int row = it * 8 + (lane >> 3), ch = lane & 7;
    bf16x8 v = *reinterpret_cast<const bf16x8*>(wl + row * 144 + ch * 16);
    *reinterpret_cast<bf16x8*>(h3 + (long)(grow0 + row) * F_ + c0 + ch * 8) = v;
  }
}

__global__ __launch_bounds__(256) void k4_gemm(const u16* __restrict__ obuf, const u16* __restrict__ wo,
                                               const u16* __restrict__ h3, const u16* __restrict__ w4,
                                               const u16* __restrict__ x1, const float* __restrict__ gate,
                                               u16* __restrict__ out) {
  extern __shared__ __attribute__((aligned(16))) char smem[];
  constexpr int BM = 128, BN = 64, WM = 2, WN = 2, NW = 4, NS = 3;
  int b = blockIdx.x, tid = threadIdx.x, lane = tid & 63;
  int wid = __builtin_amdgcn_readfirstlane(tid >> 6);
  int ct = (b & 1) * 8 + (b >> 5), rb = ((b >> 1) & 3) * 4 + ((b >> 3) & 3);
  const int wr = wid >> 1, wc = wid & 1, fr = lane & 15, fq = lane >> 4;
  const u16* asrc[BM / 8 / NW];
  const u16* bsrc[BN / 8 / NW];
  f32x4 acca[4][2], acch[4][2];
#pragma unroll
  for (int m = 0; m < 4; ++m)
#pragma unroll
    for (int n = 0; n < 2; ++n) { acca[m][n] = f32x4{0.f, 0.f, 0.f, 0.f}; acch[m][n] = f32x4{0.f, 0.f, 0.f, 0.f}; }
  float st_s = 0.f, st_ss = 0.f, st_c[8] = {0, 0, 0, 0, 0, 0, 0, 0};
  make_asrc<BM, NW>(asrc, obuf + (long)rb * BM * D_, D_, wid, lane);
  make_bsrc<BN, NW>(bsrc, wo, D_, ct * BN, wid, lane);
  mainloop<BM, BN, WM, WN, NS, false, false>(acca, asrc, D_, bsrc, D_, D_, smem, wid, lane, st_s, st_ss, st_c);
  make_asrc<BM, NW>(asrc, h3 + (long)rb * BM * F_, F_, wid, lane);
  make_bsrc<BN, NW>(bsrc, w4, D_, ct * BN, wid, lane);
  mainloop<BM, BN, WM, WN, NS, false, false>(acch, asrc, F_, bsrc, D_, F_, smem, wid, lane, st_s, st_ss, st_c);
  const int grow0 = rb * BM + wr * 64, c0 = ct * BN + wc * 32;
  char* wl = smem + wid * 10240;
#pragma unroll
  for (int m = 0; m < 4; ++m)
#pragma unroll
    for (int n = 0; n < 2; ++n)
#pragma unroll
      for (int r = 0; r < 4; ++r) {
        *reinterpret_cast<u16*>(wl + (m * 16 + fq * 4 + r) * 80 + (n * 16 + fr) * 2) = f2bf(acca[m][n][r]);
        *reinterpret_cast<u16*>(wl + 5120 + (m * 16 + fq * 4 + r) * 80 + (n * 16 + fr) * 2) = f2bf(acch[m][n][r]);
      }
#pragma unroll
  for (int it = 0; it < 4; ++it) {
    int row = it * 16 + (lane >> 2), ch = lane & 3;
    bf16x8 av = *reinterpret_cast<const bf16x8*>(wl + row * 80 + ch * 16);
    bf16x8 hv = *reinterpret_cast<const bf16x8*>(wl + 5120 + row * 80 + ch * 16);
    long idx = (long)(grow0 + row) * D_ + c0 + ch * 8;
    bf16x8 xv = *reinterpret_cast<const bf16x8*>(x1 + idx);
    float g = gate[grow0 + row];
    float og = rbf(1.f - g);
    bf16x8 o;
#pragma unroll
    for (int j = 0; j < 8; ++j) {
      float bb = rbf(bf2f((u16)xv[j]) + bf2f((u16)hv[j]));
      o[j] = (short)f2bf(rbf(g * bf2f((u16)av[j])) + rbf(og * bb));
    }
    *reinterpret_cast<bf16x8*>(out + idx) = o;
  }
}

extern "C" void kernel_launch(void* const* d_in, const int* in_sizes, int n_in, void* d_out, int out_size,
                              void* d_ws, size_t ws_size, hipStream_t stream) {
  const u16* x = (const u16*)d_in[0];
  const u16* wqkv = (const u16*)d_in[1];
  const u16* wphi = (const u16*)d_in[2];
  const u16* wo = (const u16*)d_in[3];
  const u16* w1 = (const u16*)d_in[5];
  const u16* w2 = (const u16*)d_in[6];
  const u16* w3 = (const u16*)d_in[7];
  const u16* w4 = (const u16*)d_in[8];
  const u16* gw = (const u16*)d_in[9];
  char* ws = (char*)d_ws;
  u16* phiq = (u16*)(ws + OFF_PHIQ);
  float* kvs = (float*)(ws + OFF_KVS);
  float* zs = (float*)(ws + OFF_ZS);
  u16* hbuf = (u16*)(ws + OFF_H);
  u16* x1 = (u16*)(ws + OFF_X1);
  float* st2 = (float*)(ws + OFF_ST2);
  u16* obuf = (u16*)(ws + OFF_O);
  u16* h3 = (u16*)(ws + OFF_H3);
  float* gate = (float*)(ws + OFF_GATE);
  float* cs3 = (float*)(ws + OFF_CS3);
  (void)hipFuncSetAttribute((const void*)k1_gemm, hipFuncAttributeMaxDynamicSharedMemorySize, K1_LDS);
  (void)hipFuncSetAttribute((const void*)k2_gemm, hipFuncAttributeMaxDynamicSharedMemorySize, K2_LDS);
  (void)hipFuncSetAttribute((const void*)k3_gemm, hipFuncAttributeMaxDynamicSharedMemorySize, K3_LDS);
  (void)hipFuncSetAttribute((const void*)k4_gemm, hipFuncAttributeMaxDynamicSharedMemorySize, K2_LDS);
  k1_gemm<<<256 + 61, 512, K1_LDS, stream>>>(x, wqkv, wphi, w1, w3, gw, phiq, kvs, zs, hbuf, gate, cs3);
  k2_gemm<<<256 + 64, 256, K2_LDS, stream>>>(x, hbuf, w2, x1, st2, phiq, kvs, zs, obuf);
  k3_gemm<<<224, 512, K3_LDS, stream>>>(x1, w3, st2, cs3, h3);
  k4_gemm<<<256, 256, K2_LDS, stream>>>(obuf, wo, h3, w4, x1, gate, (u16*)d_out);
}
```
